```python
import torch

def _softmax(x, axis=-1):
    return torch.softmax(x, dim=axis)
def _gelu(x):
    return torch.nn.functional.gelu(x, approximate="tanh")
def _silu(x):
    return torch.nn.functional.silu(x)
def _sigmoid(x):
    return torch.sigmoid(x)
def _layernorm(x, axis=-1, eps=1e-5):
    m = x.mean(dim=axis, keepdim=True)
    v = x.var(dim=axis, keepdim=True, unbiased=False)
    return (x - m) / torch.sqrt(v + eps)
def _scatter_add(tgt, idx, src, axis=0):
    return tgt.clone().index_add_(axis, idx, src)
def _segment_sum(src, seg, n):
    out = torch.zeros((n,) + src.shape[1:], dtype=src.dtype)
    return out.index_add_(0, seg, src)
def _pad(x, pw):
    flat = tuple(v for pair in reversed(pw) for v in pair)
    return torch.nn.functional.pad(x, flat)

kernel_name = 'standalone__softmax_full_bidir__decode'

def setup_inputs():

    _w = (lambda a: (lambda t: t.to(torch.bfloat16) if t.is_floating_point() else t)(torch.as_tensor(a)))
    B, H, Sq, Skv, Dh = 1, 32, 1, 32768, 192
    return {
        "q": _w((lambda *s: torch.randn(*s, dtype=torch.bfloat16) * 0.8)(B, H, Sq, Dh)),
        "k": _w((lambda *s: torch.randn(*s, dtype=torch.bfloat16) * 0.8)(B, H, Skv, Dh)),
        "v": _w((lambda *s: torch.randn(*s, dtype=torch.bfloat16) * 0.05)(B, H, Skv, Dh)),
    }

def reference(q, k, v):
    scale = q.shape[-1] ** -0.5
    scores = torch.einsum('bhqd,bhkd->bhqk', q, k) * scale
    attn = _softmax(scores, axis=-1)
    return torch.einsum('bhqk,bhkd->bhqd', attn, v)
```

```cpp
#define CFG_WAVES 4
#define CFG_NSPLIT 32
#include <hip/hip_runtime.h>
#include <hip/hip_bf16.h>
#include <stdint.h>

#define CFG_DEPTH 4
#define CFG_INTERLEAVE 1
#define CFG_MINWAVES 2

namespace {

constexpr int H = 32, SKV = 32768, D = 192;
constexpr int WAVES = CFG_WAVES, NSPLIT = CFG_NSPLIT, DEPTH = CFG_DEPTH;
constexpr int NTHR = WAVES * 64;
constexpr int KEYS_WG = SKV / NSPLIT;
constexpr int KEYS_WAVE = KEYS_WG / WAVES;
constexpr int NT = KEYS_WAVE / 8;
constexpr int TILE_BYTES = 8 * D * 2;
constexpr int REC = 4 + D;
static_assert(NT % DEPTH == 0 && NT >= 2 * DEPTH, "tiles per wave must be a multiple of the prefetch depth");
constexpr float C2 = 0.07216878364870322f * 1.4426950408889634f;

using u32x4 = __attribute__((ext_vector_type(4))) unsigned;
using f32x4 = __attribute__((ext_vector_type(4))) float;
using bf16x2 = __attribute__((ext_vector_type(2))) __bf16;

__device__ __forceinline__ float dot2(unsigned a, unsigned b, float c) {
  return __builtin_amdgcn_fdot2_f32_bf16(__builtin_bit_cast(bf16x2, a), __builtin_bit_cast(bf16x2, b), c, false);
}
template <int CTRL> __device__ __forceinline__ float dpp(float x) {
  return __builtin_bit_cast(float, __builtin_amdgcn_mov_dpp(__builtin_bit_cast(int, x), CTRL, 0xf, 0xf, true));
}
constexpr int XOR1 = 0xB1, XOR2 = 0x4E, HMIRROR = 0x141, ROR8 = 0x128;

__device__ __forceinline__ float x16_max(float x) {
  auto s = __builtin_amdgcn_permlane16_swap(__float_as_uint(x), __float_as_uint(x), false, false);
  x = fmaxf(__uint_as_float(s[0]), __uint_as_float(s[1]));
  auto t = __builtin_amdgcn_permlane32_swap(__float_as_uint(x), __float_as_uint(x), false, false);
  return fmaxf(__uint_as_float(t[0]), __uint_as_float(t[1]));
}
__device__ __forceinline__ float x16_sum(float x) {
  auto s = __builtin_amdgcn_permlane16_swap(__float_as_uint(x), __float_as_uint(x), false, false);
  x = __uint_as_float(s[0]) + __uint_as_float(s[1]);
  auto t = __builtin_amdgcn_permlane32_swap(__float_as_uint(x), __float_as_uint(x), false, false);
  return __uint_as_float(t[0]) + __uint_as_float(t[1]);
}

struct Stage { u32x4 k[3]; u32x4 v[3]; };

__device__ __forceinline__ void issue(Stage& st, const char* kp, const char* vp) {
#pragma unroll
  for (int j = 0; j < 3; ++j) st.k[j] = __builtin_nontemporal_load(reinterpret_cast<const u32x4*>(kp + 128 * j));
#pragma unroll
  for (int j = 0; j < 3; ++j) st.v[j] = __builtin_nontemporal_load(reinterpret_cast<const u32x4*>(vp + 128 * j));
  __builtin_amdgcn_sched_barrier(0);
}

__device__ __forceinline__ void consume(const Stage& st, const u32x4 (&q)[3], float& m, float& l, float (&acc)[24]) {
  float s0 = 0.f, s1 = 0.f, s2 = 0.f;
#pragma unroll
  for (int w = 0; w < 4; ++w) {
    s0 = dot2(st.k[0][w], q[0][w], s0);
    s1 = dot2(st.k[1][w], q[1][w], s1);
    s2 = dot2(st.k[2][w], q[2][w], s2);
  }
  float s = (s0 + s1) + s2;
  s += dpp<XOR1>(s);
  s += dpp<XOR2>(s);
  s += dpp<HMIRROR>(s);
  s *= C2;
  const float mn = fmaxf(m, s);
  const float alpha = __builtin_amdgcn_exp2f(m - mn);
  const float p = __builtin_amdgcn_exp2f(s - mn);
  l = fmaf(l, alpha, p);
  m = mn;
#pragma unroll
  for (int j = 0; j < 3; ++j)
#pragma unroll
    for (int w = 0; w < 4; ++w) {
      const unsigned x = st.v[j][w];
      const float lo = __uint_as_float(x << 16), hi = __uint_as_float(x & 0xffff0000u);
      acc[j * 8 + 2 * w] = fmaf(p, lo, acc[j * 8 + 2 * w] * alpha);
      acc[j * 8 + 2 * w + 1] = fmaf(p, hi, acc[j * 8 + 2 * w + 1] * alpha);
    }
}

__global__ void __launch_bounds__(NTHR, CFG_MINWAVES)
attn_split(const unsigned short* __restrict__ q, const unsigned short* __restrict__ k,
           const unsigned short* __restrict__ v, float* __restrict__ ws) {
  __shared__ __attribute__((aligned(16))) float lds[WAVES * REC];
  const int split = blockIdx.x % NSPLIT, head = blockIdx.x / NSPLIT;
  const int tid = threadIdx.x, lane = tid & 63, wave = __builtin_amdgcn_readfirstlane(tid >> 6);
  const int r = lane >> 3, c = lane & 7;

  u32x4 qf[3];
#pragma unroll
  for (int j = 0; j < 3; ++j)
    qf[j] = *reinterpret_cast<const u32x4*>(reinterpret_cast<const char*>(q) + (size_t)head * D * 2 + 128 * j + 16 * c);

  const size_t base = ((size_t)head * SKV + (size_t)split * KEYS_WG + (size_t)wave * 8) * (D * 2);
  constexpr size_t TSTRIDE = (size_t)WAVES * TILE_BYTES;
  const size_t loff = (size_t)r * (D * 2) + 16 * c;
  const char* kp = reinterpret_cast<const char*>(k) + base + loff;
  const char* vp = reinterpret_cast<const char*>(v) + base + loff;

  float m = -INFINITY, l = 0.f, acc[24];
#pragma unroll
  for (int i = 0; i < 24; ++i) acc[i] = 0.f;

  Stage st[DEPTH];
  __builtin_amdgcn_sched_barrier(0);
#pragma unroll
  for (int s = 0; s < DEPTH; ++s) issue(st[s], kp + s * TSTRIDE, vp + s * TSTRIDE);
  kp += DEPTH * TSTRIDE;
  vp += DEPTH * TSTRIDE;
  for (int t = 0; t < NT - DEPTH; t += DEPTH) {
#pragma unroll
    for (int s = 0; s < DEPTH; ++s) {
      consume(st[s], qf, m, l, acc);
      issue(st[s], kp + s * TSTRIDE, vp + s * TSTRIDE);
    }
    kp += DEPTH * TSTRIDE;
    vp += DEPTH * TSTRIDE;
  }
#pragma unroll
  for (int s = 0; s < DEPTH; ++s) consume(st[s], qf, m, l, acc);

  float M = fmaxf(m, dpp<ROR8>(m));
  M = x16_max(M);
  const float sc = __builtin_amdgcn_exp2f(m - M);
  l *= sc;
  l += dpp<ROR8>(l);
  l = x16_sum(l);
#pragma unroll
  for (int i = 0; i < 24; ++i) {
    float a = acc[i] * sc;
    a += dpp<ROR8>(a);
    acc[i] = x16_sum(a);
  }
  float* rec = lds + wave * REC;
  if (r == 0) {
#pragma unroll
    for (int j = 0; j < 3; ++j) {
      *reinterpret_cast<f32x4*>(rec + 4 + 64 * j + 8 * c) = f32x4{acc[8 * j], acc[8 * j + 1], acc[8 * j + 2], acc[8 * j + 3]};
      *reinterpret_cast<f32x4*>(rec + 4 + 64 * j + 8 * c + 4) = f32x4{acc[8 * j + 4], acc[8 * j + 5], acc[8 * j + 6], acc[8 * j + 7]};
    }
    if (c == 0) { rec[0] = M; rec[1] = l; }
  }
  __syncthreads();
  if (tid < D) {
    float Mw = -INFINITY;
#pragma unroll
    for (int w = 0; w < WAVES; ++w) Mw = fmaxf(Mw, lds[w * REC]);
    float L = 0.f, o = 0.f;
#pragma unroll
    for (int w = 0; w < WAVES; ++w) {
      const float wt = __builtin_amdgcn_exp2f(lds[w * REC] - Mw);
      L = fmaf(wt, lds[w * REC + 1], L);
      o = fmaf(wt, lds[w * REC + 4 + tid], o);
    }
    float* out = ws + (size_t)blockIdx.x * REC;
    out[4 + tid] = o;
    if (tid == 0) { out[0] = Mw; out[1] = L; }
  }
}

__global__ void __launch_bounds__(D)
attn_combine(const float* __restrict__ ws, __hip_bfloat16* __restrict__ out) {
  const int head = blockIdx.x, d = threadIdx.x;
  const float* p = ws + (size_t)head * NSPLIT * REC;
  float M = -INFINITY;
#pragma unroll
  for (int s = 0; s < NSPLIT; ++s) M = fmaxf(M, p[s * REC]);
  float L = 0.f, o = 0.f;
#pragma unroll
  for (int s = 0; s < NSPLIT; ++s) {
    const float wt = __builtin_amdgcn_exp2f(p[s * REC] - M);
    L = fmaf(wt, p[s * REC + 1], L);
    o = fmaf(wt, p[s * REC + 4 + d], o);
  }
  out[head * D + d] = __float2bfloat16(o / L);
}

}

extern "C" void kernel_launch(void* const* d_in, const int* in_sizes, int n_in,
                              void* d_out, int out_size, void* d_ws, size_t ws_size,
                              hipStream_t stream) {
  const unsigned short* q = (const unsigned short*)d_in[0];
  const unsigned short* k = (const unsigned short*)d_in[1];
  const unsigned short* v = (const unsigned short*)d_in[2];
  attn_split<<<H * NSPLIT, NTHR, 0, stream>>>(q, k, v, (float*)d_ws);
  attn_combine<<<H, D, 0, stream>>>((const float*)d_ws, (__hip_bfloat16*)d_out);
}
```

```cpp
#include <hip/hip_runtime.h>
#include <hip/hip_bf16.h>
#include <stdint.h>

namespace {

constexpr int H = 32, SKV = 32768, D = 192;
constexpr int WAVES = 8, NSPLIT = 8, DEPTH = 2;
constexpr int NTHR = WAVES * 64;
constexpr int KEYS_WG = SKV / NSPLIT;
constexpr int NT = KEYS_WG / WAVES / 8;
constexpr int TILE_BYTES = 8 * D * 2;
constexpr int REC = 4 + D;
constexpr unsigned KV_BYTES = (unsigned)H * SKV * D * 2;
static_assert((NT & (NT - 1)) == 0, "NT must be a power of two");
static_assert(NT % DEPTH == 0 && NT >= 2 * DEPTH, "tiles per wave must be a multiple of the prefetch depth");
constexpr float C2 = 0.07216878364870322f * 1.4426950408889634f;

using u32x4 = __attribute__((ext_vector_type(4))) unsigned;
using f32x4 = __attribute__((ext_vector_type(4))) float;
using bf16x2 = __attribute__((ext_vector_type(2))) __bf16;

__device__ __forceinline__ float dot2(unsigned a, unsigned b, float c) {
  return __builtin_amdgcn_fdot2_f32_bf16(__builtin_bit_cast(bf16x2, a), __builtin_bit_cast(bf16x2, b), c, false);
}
template <int CTRL> __device__ __forceinline__ float dpp(float x) {
  return __builtin_bit_cast(float, __builtin_amdgcn_mov_dpp(__builtin_bit_cast(int, x), CTRL, 0xf, 0xf, true));
}
constexpr int XOR1 = 0xB1, XOR2 = 0x4E, HMIRROR = 0x141, ROR8 = 0x128;

__device__ __forceinline__ float x16_max(float x) {
  auto s = __builtin_amdgcn_permlane16_swap(__float_as_uint(x), __float_as_uint(x), false, false);
  x = fmaxf(__uint_as_float(s[0]), __uint_as_float(s[1]));
  auto t = __builtin_amdgcn_permlane32_swap(__float_as_uint(x), __float_as_uint(x), false, false);
  return fmaxf(__uint_as_float(t[0]), __uint_as_float(t[1]));
}
__device__ __forceinline__ float x16_sum(float x) {
  auto s = __builtin_amdgcn_permlane16_swap(__float_as_uint(x), __float_as_uint(x), false, false);
  x = __uint_as_float(s[0]) + __uint_as_float(s[1]);
  auto t = __builtin_amdgcn_permlane32_swap(__float_as_uint(x), __float_as_uint(x), false, false);
  return __uint_as_float(t[0]) + __uint_as_float(t[1]);
}

struct Stage { u32x4 k[3]; u32x4 v[3]; };

template <typename R>
__device__ __forceinline__ void issue(Stage& st, R rk, R rv, unsigned vo, unsigned so) {
#pragma unroll
  for (int j = 0; j < 3; ++j) st.k[j] = __builtin_amdgcn_raw_buffer_load_b128(rk, vo + 128 * j, so, 2  );
#pragma unroll
  for (int j = 0; j < 3; ++j) st.v[j] = __builtin_amdgcn_raw_buffer_load_b128(rv, vo + 128 * j, so, 2  );
  __builtin_amdgcn_sched_barrier(0);
}

__device__ __forceinline__ void consume(const Stage& st, const u32x4 (&q)[3], float& m, float& l, float (&acc)[24]) {
  float s0 = 0.f, s1 = 0.f, s2 = 0.f;
#pragma unroll
  for (int w = 0; w < 4; ++w) {
    s0 = dot2(st.k[0][w], q[0][w], s0);
    s1 = dot2(st.k[1][w], q[1][w], s1);
    s2 = dot2(st.k[2][w], q[2][w], s2);
  }
  float s = (s0 + s1) + s2;
  s += dpp<XOR1>(s);
  s += dpp<XOR2>(s);
  s += dpp<HMIRROR>(s);
  s *= C2;
  const float mn = fmaxf(m, s);
  const float alpha = __builtin_amdgcn_exp2f(m - mn);
  const float p = __builtin_amdgcn_exp2f(s - mn);
  l = fmaf(l, alpha, p);
  m = mn;
#pragma unroll
  for (int j = 0; j < 3; ++j)
#pragma unroll
    for (int w = 0; w < 4; ++w) {
      const unsigned x = st.v[j][w];
      const float lo = __uint_as_float(x << 16), hi = __uint_as_float(x & 0xffff0000u);
      acc[j * 8 + 2 * w] = fmaf(p, lo, acc[j * 8 + 2 * w] * alpha);
      acc[j * 8 + 2 * w + 1] = fmaf(p, hi, acc[j * 8 + 2 * w + 1] * alpha);
    }
  asm volatile("" : "+v"(acc[0]), "+v"(acc[1]), "+v"(acc[2]), "+v"(acc[3]), "+v"(acc[4]), "+v"(acc[5]), "+v"(acc[6]), "+v"(acc[7]),
                    "+v"(acc[8]), "+v"(acc[9]), "+v"(acc[10]), "+v"(acc[11]), "+v"(acc[12]), "+v"(acc[13]), "+v"(acc[14]), "+v"(acc[15]),
                    "+v"(acc[16]), "+v"(acc[17]), "+v"(acc[18]), "+v"(acc[19]), "+v"(acc[20]), "+v"(acc[21]), "+v"(acc[22]), "+v"(acc[23]),
                    "+v"(l), "+v"(m) :: "memory");
  __builtin_amdgcn_sched_barrier(0);
}

__global__ void __launch_bounds__(NTHR, 2)
attn_split(const unsigned short* __restrict__ q, const unsigned short* __restrict__ k,
           const unsigned short* __restrict__ v, float* __restrict__ ws) {
  __shared__ __attribute__((aligned(16))) float lds[WAVES * REC];
  const int split = blockIdx.x % NSPLIT, head = blockIdx.x / NSPLIT;
  const int tid = threadIdx.x, lane = tid & 63, wave = __builtin_amdgcn_readfirstlane(tid >> 6);
  const int r = lane >> 3, c = lane & 7;

  auto rk = __builtin_amdgcn_make_buffer_rsrc((void*)k, 0, KV_BYTES, 0x00020000);
  auto rv = __builtin_amdgcn_make_buffer_rsrc((void*)v, 0, KV_BYTES, 0x00020000);
  const unsigned sbase = (unsigned)((head * SKV + split * KEYS_WG + wave * 8) * (D * 2));
  constexpr unsigned TSTRIDE = WAVES * TILE_BYTES;
  unsigned ci = blockIdx.x & (NT - 1);
  const unsigned vo = r * (D * 2) + 16 * c;

  u32x4 qf[3];
#pragma unroll
  for (int j = 0; j < 3; ++j)
    qf[j] = *reinterpret_cast<const u32x4*>(reinterpret_cast<const char*>(q) + (size_t)head * D * 2 + 128 * j + 16 * c);
  __builtin_amdgcn_sched_barrier(0);

  Stage st[DEPTH];
#pragma unroll
  for (int s = 0; s < DEPTH; ++s) {
    issue(st[s], rk, rv, vo, sbase + ci * TSTRIDE);
    ci = (ci + 1) & (NT - 1);
  }

  float m = -INFINITY, l = 0.f, acc[24];
#pragma unroll
  for (int i = 0; i < 24; ++i) acc[i] = 0.f;

  for (int t = 0; t < NT - DEPTH; t += DEPTH) {
#pragma unroll
    for (int s = 0; s < DEPTH; ++s) {
      consume(st[s], qf, m, l, acc);
      issue(st[s], rk, rv, vo, sbase + ci * TSTRIDE);
      ci = (ci + 1) & (NT - 1);
    }
  }
#pragma unroll
  for (int s = 0; s < DEPTH; ++s) consume(st[s], qf, m, l, acc);

  float M = fmaxf(m, dpp<ROR8>(m));
  M = x16_max(M);
  const float sc = __builtin_amdgcn_exp2f(m - M);
  l *= sc;
  l += dpp<ROR8>(l);
  l = x16_sum(l);
#pragma unroll
  for (int i = 0; i < 24; ++i) {
    float a = acc[i] * sc;
    a += dpp<ROR8>(a);
    acc[i] = x16_sum(a);
  }
  float* rec = lds + wave * REC;
  if (r == 0) {
#pragma unroll
    for (int j = 0; j < 3; ++j) {
      *reinterpret_cast<f32x4*>(rec + 4 + 64 * j + 8 * c) = f32x4{acc[8 * j], acc[8 * j + 1], acc[8 * j + 2], acc[8 * j + 3]};
      *reinterpret_cast<f32x4*>(rec + 4 + 64 * j + 8 * c + 4) = f32x4{acc[8 * j + 4], acc[8 * j + 5], acc[8 * j + 6], acc[8 * j + 7]};
    }
    if (c == 0) { rec[0] = M; rec[1] = l; }
  }
  __syncthreads();
  if (tid < D) {
    float Mw = -INFINITY;
#pragma unroll
    for (int w = 0; w < WAVES; ++w) Mw = fmaxf(Mw, lds[w * REC]);
    float L = 0.f, o = 0.f;
#pragma unroll
    for (int w = 0; w < WAVES; ++w) {
      const float wt = __builtin_amdgcn_exp2f(lds[w * REC] - Mw);
      L = fmaf(wt, lds[w * REC + 1], L);
      o = fmaf(wt, lds[w * REC + 4 + tid], o);
    }
    float* out = ws + (size_t)blockIdx.x * REC;
    out[4 + tid] = o;
    if (tid == 0) { out[0] = Mw; out[1] = L; }
  }
}

__global__ void __launch_bounds__(D)
attn_combine(const float* __restrict__ ws, __hip_bfloat16* __restrict__ out) {
  const int head = blockIdx.x, d = threadIdx.x;
  const float* p = ws + (size_t)head * NSPLIT * REC;
  float M = -INFINITY;
#pragma unroll
  for (int s = 0; s < NSPLIT; ++s) M = fmaxf(M, p[s * REC]);
  float L = 0.f, o = 0.f;
#pragma unroll
  for (int s = 0; s < NSPLIT; ++s) {
    const float wt = __builtin_amdgcn_exp2f(p[s * REC] - M);
    L = fmaf(wt, p[s * REC + 1], L);
    o = fmaf(wt, p[s * REC + 4 + d], o);
  }
  out[head * D + d] = __float2bfloat16(o / L);
}

}

extern "C" void kernel_launch(void* const* d_in, const int* in_sizes, int n_in,
                              void* d_out, int out_size, void* d_ws, size_t ws_size,
                              hipStream_t stream) {
  const unsigned short* q = (const unsigned short*)d_in[0];
  const unsigned short* k = (const unsigned short*)d_in[1];
  const unsigned short* v = (const unsigned short*)d_in[2];
  attn_split<<<H * NSPLIT, NTHR, 0, stream>>>(q, k, v, (float*)d_ws);
  attn_combine<<<H, D, 0, stream>>>((const float*)d_ws, (__hip_bfloat16*)d_out);
}
```
